```python
import torch

def _softmax(x, axis=-1):
    return torch.softmax(x, dim=axis)
def _gelu(x):
    return torch.nn.functional.gelu(x, approximate="tanh")
def _silu(x):
    return torch.nn.functional.silu(x)
def _sigmoid(x):
    return torch.sigmoid(x)
def _layernorm(x, axis=-1, eps=1e-5):
    m = x.mean(dim=axis, keepdim=True)
    v = x.var(dim=axis, keepdim=True, unbiased=False)
    return (x - m) / torch.sqrt(v + eps)
def _scatter_add(tgt, idx, src, axis=0):
    return tgt.clone().index_add_(axis, idx, src)
def _segment_sum(src, seg, n):
    out = torch.zeros((n,) + src.shape[1:], dtype=src.dtype)
    return out.index_add_(0, seg, src)
def _pad(x, pw):
    flat = tuple(v for pair in reversed(pw) for v in pair)
    return torch.nn.functional.pad(x, flat)

kernel_name = 'standalone__grn__sampling'

def setup_inputs():

    _w = (lambda a: (lambda t: t.to(torch.bfloat16) if t.is_floating_point() else t)(torch.as_tensor(a)))
    B, S, D = 1024, 16, 640
    return {
        "x": _w((lambda *s: torch.randn(*s, dtype=torch.bfloat16) * 0.05)(B, S, D)),
        "g": _w((lambda *s: torch.randn(*s, dtype=torch.bfloat16) * 0.4)(D) * 0.1),
        "b": _w((lambda *s: torch.randn(*s, dtype=torch.bfloat16) * 0.1)(D) * 0.1),
    }

def reference(x, g, b):
    gx = torch.sqrt((x * x).sum(axis=-2, keepdims=True))
    nx = gx / (gx.mean(axis=-1, keepdims=True) + 1e-6)
    return g * (x * nx) + b + x
```

```cpp
#include <hip/hip_runtime.h>
#include <hip/hip_bf16.h>
#include <stdint.h>

typedef unsigned int u32;
typedef __attribute__((ext_vector_type(4))) unsigned int u32x4;
typedef __attribute__((ext_vector_type(2))) float f32x2;

constexpr int S_ = 16, D_ = 640;

__device__ __forceinline__ float bf_lo(u32 v) { return __uint_as_float(v << 16); }
__device__ __forceinline__ float bf_hi(u32 v) { return __uint_as_float(v & 0xffff0000u); }
__device__ __forceinline__ u32 pack_bf16(float lo, float hi) {
    typedef __attribute__((ext_vector_type(2))) __bf16 bf2;
    bf2 r; r[0] = (__bf16)lo; r[1] = (__bf16)hi;
    return __builtin_bit_cast(u32, r);
}

__device__ __forceinline__ float dpp_add_xor(float v, int) { return v; }

__global__ __launch_bounds__(320) void grn_kernel(const u32x4* __restrict__ x, const u32x4* __restrict__ g,
                                                  const u32x4* __restrict__ b, u32x4* __restrict__ out) {
    __shared__ float red[8];
    const int t = threadIdx.x;
    const int w = t >> 6, l = t & 63;
    const int cc = w * 16 + (l & 15);
    const int rg = l >> 4;
    const size_t base = (size_t)blockIdx.x * (S_ * D_ / 8);
    const u32x4* xp = x + base + rg * 80 + cc;
    u32x4 v[4];
#pragma unroll
    for (int i = 0; i < 4; ++i) v[i] = __builtin_nontemporal_load(xp + i * 320);
    u32x4 gv = g[cc];
    u32x4 bv = b[cc];

    float p[8];
#pragma unroll
    for (int k = 0; k < 8; ++k) p[k] = 0.f;
#pragma unroll
    for (int i = 0; i < 4; ++i) {
#pragma unroll
        for (int k = 0; k < 4; ++k) {
            float lo = bf_lo(v[i][k]), hi = bf_hi(v[i][k]);
            p[2 * k] = fmaf(lo, lo, p[2 * k]);
            p[2 * k + 1] = fmaf(hi, hi, p[2 * k + 1]);
        }
    }
    float q[4];
#pragma unroll
    for (int k = 0; k < 4; ++k) {
        auto r = __builtin_amdgcn_permlane32_swap(__float_as_uint(p[k]), __float_as_uint(p[k + 4]), false, false);
        q[k] = __uint_as_float(r[0]) + __uint_as_float(r[1]);
    }
    float s2[2];
#pragma unroll
    for (int k = 0; k < 2; ++k) {
        auto r = __builtin_amdgcn_permlane16_swap(__float_as_uint(q[k]), __float_as_uint(q[k + 2]), false, false);
        s2[k] = __uint_as_float(r[0]) + __uint_as_float(r[1]);
    }
    float gx0 = __builtin_amdgcn_sqrtf(s2[0]);
    float gx1 = __builtin_amdgcn_sqrtf(s2[1]);
    float ws = gx0 + gx1;
    for (int o = 32; o > 0; o >>= 1) ws += __shfl_xor(ws, o, 64);
    if (l == 0) red[w] = ws;
    float h[4];
    {
        auto r0 = __builtin_amdgcn_permlane16_swap(__float_as_uint(gx0), __float_as_uint(gx0), false, false);
        auto r1 = __builtin_amdgcn_permlane16_swap(__float_as_uint(gx1), __float_as_uint(gx1), false, false);
        h[0] = __uint_as_float(r0[0]); h[2] = __uint_as_float(r0[1]);
        h[1] = __uint_as_float(r1[0]); h[3] = __uint_as_float(r1[1]);
    }
    float gx[8];
#pragma unroll
    for (int k = 0; k < 4; ++k) {
        auto r = __builtin_amdgcn_permlane32_swap(__float_as_uint(h[k]), __float_as_uint(h[k]), false, false);
        gx[k] = __uint_as_float(r[0]); gx[k + 4] = __uint_as_float(r[1]);
    }
    float gg[8], bb[8];
#pragma unroll
    for (int k = 0; k < 4; ++k) {
        gg[2 * k] = bf_lo(gv[k]) * gx[2 * k];
        gg[2 * k + 1] = bf_hi(gv[k]) * gx[2 * k + 1];
        bb[2 * k] = bf_lo(bv[k]);
        bb[2 * k + 1] = bf_hi(bv[k]);
    }
    __syncthreads();
    float tot = red[0] + red[1] + red[2] + red[3] + red[4];
    float inv = 1.0f / (tot * (1.0f / D_) + 1e-6f);
    float sc[8];
#pragma unroll
    for (int k = 0; k < 8; ++k) sc[k] = fmaf(gg[k], inv, 1.0f);
    u32x4* op = out + base + rg * 80 + cc;
#pragma unroll
    for (int i = 0; i < 4; ++i) {
        u32x4 o;
#pragma unroll
        for (int k = 0; k < 4; ++k) {
            float lo = fmaf(bf_lo(v[i][k]), sc[2 * k], bb[2 * k]);
            float hi = fmaf(bf_hi(v[i][k]), sc[2 * k + 1], bb[2 * k + 1]);
            o[k] = pack_bf16(lo, hi);
        }
        __builtin_nontemporal_store(o, op + i * 320);
    }
}

extern "C" void kernel_launch(void* const* d_in, const int* in_sizes, int n_in,
                              void* d_out, int out_size, void* d_ws, size_t ws_size,
                              hipStream_t stream) {
    const int B = in_sizes[0] / (S_ * D_);
    grn_kernel<<<B, 320, 0, stream>>>((const u32x4*)d_in[0], (const u32x4*)d_in[1], (const u32x4*)d_in[2], (u32x4*)d_out);
}
```

```cpp
#include <hip/hip_runtime.h>
#include <stdint.h>

typedef unsigned int u32;
typedef __attribute__((ext_vector_type(4))) unsigned int u32x4;
typedef __attribute__((ext_vector_type(2))) float f32x2;

#define SCHED_FENCE() __builtin_amdgcn_sched_barrier(0)

constexpr int S_ = 16, D_ = 640;
constexpr u32 ROWB = D_ * 2;
constexpr u32 EB = S_ * ROWB;
constexpr int AUX_NT = 2;

__device__ __forceinline__ float bf_lo(u32 v) { return __uint_as_float(v << 16); }
__device__ __forceinline__ float bf_hi(u32 v) { return __uint_as_float(v & 0xffff0000u); }
__device__ __forceinline__ u32 pack_bf16(float lo, float hi) {
    typedef __attribute__((ext_vector_type(2))) __bf16 bf2;
    bf2 r;
    r[0] = (__bf16)lo;
    r[1] = (__bf16)hi;
    return __builtin_bit_cast(u32, r);
}
template <int CTRL>
__device__ __forceinline__ float dpp_add(float v) {
    int r = __builtin_amdgcn_update_dpp(0, __float_as_int(v), CTRL, 0xf, 0xf, true);
    return v + __int_as_float(r);
}
__device__ __forceinline__ void swap32(float& a, float& b) {
    auto r = __builtin_amdgcn_permlane32_swap(__float_as_uint(a), __float_as_uint(b), false, false);
    a = __uint_as_float(r[0]);
    b = __uint_as_float(r[1]);
}
__device__ __forceinline__ void swap16(float& a, float& b) {
    auto r = __builtin_amdgcn_permlane16_swap(__float_as_uint(a), __float_as_uint(b), false, false);
    a = __uint_as_float(r[0]);
    b = __uint_as_float(r[1]);
}

__device__ __forceinline__ void grn_elem(const u32x4 (&v)[4], const f32x2 (&bb)[4], float g0, float g1,
                                         float* red, u32 slab, u32x4 (&o)[4]) {
    f32x2 f[4][4];
    f32x2 p[4];
#pragma unroll
    for (int i = 0; i < 4; ++i)
#pragma unroll
        for (int k = 0; k < 4; ++k) {
            f[i][k] = f32x2{bf_lo(v[i][k]), bf_hi(v[i][k])};
            p[k] = i == 0 ? f[i][k] * f[i][k] : __builtin_elementwise_fma(f[i][k], f[i][k], p[k]);
        }
    float a0 = p[0][0], a1 = p[0][1], a2 = p[1][0], a3 = p[1][1];
    float a4 = p[2][0], a5 = p[2][1], a6 = p[3][0], a7 = p[3][1];
    swap32(a0, a4); swap32(a1, a5); swap32(a2, a6); swap32(a3, a7);
    a0 += a4; a1 += a5; a2 += a6; a3 += a7;
    swap16(a0, a2); swap16(a1, a3);
    const float gx0 = __builtin_amdgcn_sqrtf(a0 + a2);
    const float gx1 = __builtin_amdgcn_sqrtf(a1 + a3);
    float ws = gx0 + gx1;
    ws = dpp_add<0xB1>(ws);
    ws = dpp_add<0x4E>(ws);
    ws = dpp_add<0x141>(ws);
    ws = dpp_add<0x140>(ws);
    {
        float c = ws;
        swap16(ws, c); ws += c;
        c = ws;
        swap32(ws, c); ws += c;
    }
    red[slab] = ws;
    float h0 = g0 * gx0, h1 = g1 * gx1, h2 = h0, h3 = h1;
    swap16(h0, h2); swap16(h1, h3);
    float h4 = h0, h5 = h1, h6 = h2, h7 = h3;
    swap32(h0, h4); swap32(h1, h5); swap32(h2, h6); swap32(h3, h7);
    __syncthreads();
    const float tot = ((red[0] + red[1]) + (red[2] + red[3])) + red[4];
    const float inv = __builtin_amdgcn_rcpf(fmaf(tot, 1.0f / D_, 1e-6f));
    const f32x2 inv2 = {inv, inv}, one2 = {1.0f, 1.0f};
    f32x2 sc[4];
    sc[0] = __builtin_elementwise_fma(f32x2{h0, h1}, inv2, one2);
    sc[1] = __builtin_elementwise_fma(f32x2{h2, h3}, inv2, one2);
    sc[2] = __builtin_elementwise_fma(f32x2{h4, h5}, inv2, one2);
    sc[3] = __builtin_elementwise_fma(f32x2{h6, h7}, inv2, one2);
#pragma unroll
    for (int i = 0; i < 4; ++i)
#pragma unroll
        for (int k = 0; k < 4; ++k) {
            f32x2 y = __builtin_elementwise_fma(f[i][k], sc[k], bb[k]);
            o[i][k] = pack_bf16(y[0], y[1]);
        }
}

__global__ __launch_bounds__(640) __attribute__((aligned(4096)))
void grn_kernel(const char* __restrict__ x, const char* __restrict__ g, const char* __restrict__ b,
                char* __restrict__ out, u32 half, u32 total) {
    __shared__ float red[32];
    const u32 t = threadIdx.x;
    const u32 w = t >> 6, l = t & 63;
    const u32 grp = w >= 5 ? 1 : 0;
    const u32 slab = w - grp * 5;
    const u32 coff = slab * 256 + (l & 15) * 16;
    const u32 voff0 = (l >> 4) * (4 * ROWB) + coff + grp * EB + blockIdx.x * (2 * EB);
    const u32 voff1 = voff0 + half;
    __amdgpu_buffer_rsrc_t xr = __builtin_amdgcn_make_buffer_rsrc((void*)x, 0, total, 0x00020000);
    __amdgpu_buffer_rsrc_t orr = __builtin_amdgcn_make_buffer_rsrc(out, 0, total, 0x00020000);

    u32x4 v0[4], v1[4];
#pragma unroll
    for (int i = 0; i < 4; ++i) v0[i] = __builtin_amdgcn_raw_buffer_load_b128(xr, voff0 + i * ROWB, 0, AUX_NT);
    SCHED_FENCE();
    const u32 gv = *(const u32*)(g + (coff + (l >> 4) * 4));
    const u32x4 bv = *(const u32x4*)(b + coff);
    SCHED_FENCE();
    __builtin_amdgcn_s_barrier();
    SCHED_FENCE();
#pragma unroll
    for (int i = 0; i < 4; ++i) v1[i] = __builtin_amdgcn_raw_buffer_load_b128(xr, voff1 + i * ROWB, 0, AUX_NT);
    SCHED_FENCE();

    f32x2 bb[4];
#pragma unroll
    for (int k = 0; k < 4; ++k) bb[k] = f32x2{bf_lo(bv[k]), bf_hi(bv[k])};
    const float g0 = bf_lo(gv), g1 = bf_hi(gv);

    u32x4 o0[4], o1[4];
    grn_elem(v0, bb, g0, g1, red + grp * 8, slab, o0);
#pragma unroll
    for (int i = 0; i < 4; ++i) __builtin_amdgcn_raw_buffer_store_b128(o0[i], orr, voff0 + i * ROWB, 0, AUX_NT);
    SCHED_FENCE();
    grn_elem(v1, bb, g0, g1, red + 16 + grp * 8, slab, o1);
#pragma unroll
    for (int i = 0; i < 4; ++i) __builtin_amdgcn_raw_buffer_store_b128(o1[i], orr, voff1 + i * ROWB, 0, AUX_NT);
}

extern "C" void kernel_launch(void* const* d_in, const int* in_sizes, int n_in,
                              void* d_out, int out_size, void* d_ws, size_t ws_size,
                              hipStream_t stream) {
    const int B = in_sizes[0] / (S_ * D_);
    const int nwg = (B + 3) / 4;
    if (nwg == 0) return;
    grn_kernel<<<nwg, 640, 0, stream>>>((const char*)d_in[0], (const char*)d_in[1], (const char*)d_in[2],
                                        (char*)d_out, (u32)nwg * 2 * EB, (u32)B * EB);
}
```
